```python
import torch

def _softmax(x, axis=-1):
    return torch.softmax(x, dim=axis)
def _gelu(x):
    return torch.nn.functional.gelu(x, approximate="tanh")
def _silu(x):
    return torch.nn.functional.silu(x)
def _sigmoid(x):
    return torch.sigmoid(x)
def _layernorm(x, axis=-1, eps=1e-5):
    m = x.mean(dim=axis, keepdim=True)
    v = x.var(dim=axis, keepdim=True, unbiased=False)
    return (x - m) / torch.sqrt(v + eps)
def _scatter_add(tgt, idx, src, axis=0):
    return tgt.clone().index_add_(axis, idx, src)
def _segment_sum(src, seg, n):
    out = torch.zeros((n,) + src.shape[1:], dtype=src.dtype)
    return out.index_add_(0, seg, src)
def _pad(x, pw):
    flat = tuple(v for pair in reversed(pw) for v in pair)
    return torch.nn.functional.pad(x, flat)

kernel_name = 'parallel_sum__linear_attn_block__dense_activation_gelu__prompt'

def setup_inputs():

    _w = (lambda a: (lambda t: t.to(torch.bfloat16) if t.is_floating_point() else t)(torch.as_tensor(a)))
    def _A_setup():
        B, S, D = 1, 2048, 512
        return {
            "x": _w((lambda *s: torch.randn(*s, dtype=torch.bfloat16) * 0.05)(B, S, D)),
            "wqkv": _w((lambda *s: torch.randn(*s, dtype=torch.bfloat16) * 0.2)(D, 3 * D)),
            "wo": _w((lambda *s: torch.randn(*s, dtype=torch.bfloat16) * 0.05)(D, D)),
        }
    _A_in = _A_setup()

    def _B_setup():
        B, S, D, F = 1, 2048, 512, 1792
        return {
            "x": _w((lambda *s: torch.randn(*s, dtype=torch.bfloat16) * 0.05)(B, S, D)),
            "w1": _w((lambda *s: torch.randn(*s, dtype=torch.bfloat16) * 0.8)(D, F) * (D ** -0.5)),
            "b1": _w((lambda *s: torch.randn(*s, dtype=torch.bfloat16) * 12.8)(F) * 0.01),
            "w2": _w((lambda *s: torch.randn(*s, dtype=torch.bfloat16) * 0.8)(F, D) * (F ** -0.5)),
            "b2": _w((lambda *s: torch.randn(*s, dtype=torch.bfloat16) * 12.8)(D) * 0.01),
        }
    _B_in = _B_setup()

    return {
        "A_x": _A_in["x"],
        "A_wqkv": _A_in["wqkv"],
        "A_wo": _A_in["wo"],
        "B_x": _B_in["x"],
        "B_w1": _B_in["w1"],
        "B_b1": _B_in["b1"],
        "B_w2": _B_in["w2"],
        "B_b2": _B_in["b2"],
    }

def reference(A_x, A_wqkv, A_wo, B_x, B_w1, B_b1, B_w2, B_b2):
    def _A_ref(x, wqkv, wo):
        B, S, D = x.shape
        H = 32; Dh = D // H
        qkv = (lambda x, *d: x.permute(*d))((x @ wqkv).reshape(B, S, 3, H, Dh), 2, 0, 3, 1, 4)
        q, k, v = qkv[0], qkv[1], qkv[2]
        phi = lambda t: torch.where(t > 0, t + 1, torch.exp(t)) + 1e-6
        pq, pk = phi(q), phi(k)
        kv = torch.einsum("bhsd,bhse->bhde", pk, v)
        z = torch.einsum("bhsd->bhd", pk)
        num = torch.einsum("bhqd,bhde->bhqe", pq, kv)
        den = torch.einsum("bhqd,bhd->bhq", pq, z)[..., None] + 1e-6
        o = num / den
        return (lambda x, *d: x.permute(*d))(o, 0, 2, 1, 3).reshape(B, S, D) @ wo

    def _B_ref(x, w1, b1, w2, b2):
        h = torch.einsum("bsd,df->bsf", x, w1) + b1
        h = _gelu(h)
        return torch.einsum("bsf,fd->bsd", h, w2) + b2

    x = A_x
    xn = _layernorm(x, axis=-1)
    return x + _A_ref(xn, A_wqkv, A_wo) + _B_ref(xn, B_w1, B_b1, B_w2, B_b2)
```

```cpp
#include <hip/hip_runtime.h>
#include <stdint.h>
#include <type_traits>
#pragma clang diagnostic ignored "-Wint-to-pointer-cast"

typedef __bf16   bf16x8 __attribute__((ext_vector_type(8)));
typedef short    s16x4  __attribute__((ext_vector_type(4)));
typedef __bf16   bf16x2 __attribute__((ext_vector_type(2)));
typedef float    f32x4  __attribute__((ext_vector_type(4)));
typedef float    f32x2  __attribute__((ext_vector_type(2)));
typedef float    f32x16 __attribute__((ext_vector_type(16)));
typedef unsigned u32x4  __attribute__((ext_vector_type(4)));
typedef unsigned u32x2  __attribute__((ext_vector_type(2)));
typedef unsigned long long u64;
#define LAS __attribute__((address_space(3)))
#define GAS __attribute__((address_space(1)))
#define RLX_AGENT __ATOMIC_RELAXED, __HIP_MEMORY_SCOPE_AGENT
#define SB() __builtin_amdgcn_sched_barrier(0)
#define CFENCE() asm volatile("" ::: "memory")
#define LGKM0() __builtin_amdgcn_s_waitcnt(0xC07F)

constexpr int LDS_BYTES = 163840;
constexpr int STAGE = 49152;
constexpr int MISC = 3 * STAGE;
constexpr int M_RSTD = MISC;
constexpr int M_MR = MISC + 512;
constexpr int M_CS = MISC + 1024;
constexpr int M_B1 = MISC + 2048;
constexpr int M_KV = MISC + 4096;
constexpr int M_SY = MISC + 12288;
constexpr int M_DUMP = MISC + 15360;
constexpr int WS_EPOCH = 0;
constexpr int FS = 256;
constexpr int WS_FLAG = 4096;
constexpr int WS_STAT = WS_FLAG + 512 * FS;
constexpr int WS_CS = WS_STAT + 32768;
constexpr int WS_KVP = WS_CS + 52 * 2048;
constexpr int KVREC = 1152;
constexpr int WS_A = 1 << 20;
constexpr int WS_END = WS_A + 64 * 144 * 1024;
static_assert(WS_KVP + 512 * KVREC <= WS_A, "workspace map");
constexpr unsigned SPIN_CAP = 1u << 20;
constexpr int PACE_H = 6, PACE_Q = 12;
enum { SY_CNT_A = 0, SY_CNT_B = 1, SY_CNT_C = 2, SY_P1 = 4, SY_P2 = 8, SY_P3 = 12, SY_LVL = 16, SY_CONS = 20, SY_LAND = 24, SY_DONE = 28, SY_PRE = 64 };

__device__ __forceinline__ void glds16(unsigned lds_off, unsigned voff, const char* sbase) {
  asm volatile("s_mov_b32 m0, %0\n\ts_nop 0\n\tglobal_load_lds_dwordx4 %1, %2" ::"s"(lds_off), "v"(voff), "s"(sbase) : "memory");
}
__device__ __forceinline__ void touch(const char* addr) {
  asm volatile("s_mov_b32 m0, %0\n\ts_nop 0\n\tglobal_load_lds_dword %1, off" :: "s"(M_DUMP), "v"(addr) : "memory");
}
__device__ __forceinline__ f32x4 mfma32(u32x4 a, u32x4 b, f32x4 c) {
  return __builtin_amdgcn_mfma_f32_16x16x32_bf16(__builtin_bit_cast(bf16x8, a), __builtin_bit_cast(bf16x8, b), c, 0, 0, 0);
}
__device__ __forceinline__ f32x16 mfma32x32(u32x4 a, u32x4 b, f32x16 c) {
  return __builtin_amdgcn_mfma_f32_32x32x16_bf16(__builtin_bit_cast(bf16x8, a), __builtin_bit_cast(bf16x8, b), c, 0, 0, 0);
}
__device__ __forceinline__ f32x4 mfma16(u32x2 a, u32x2 b, f32x4 c) {
  return __builtin_amdgcn_mfma_f32_16x16x16bf16_1k(__builtin_bit_cast(s16x4, a), __builtin_bit_cast(s16x4, b), c, 0, 0, 0);
}
__device__ __forceinline__ u32x2 trread(unsigned addr) {
  return __builtin_bit_cast(u32x2, __builtin_amdgcn_ds_read_tr16_b64_v4i16((LAS s16x4*)addr));
}
__device__ __forceinline__ u32x4 to_frag32(u32x4 v) {
  const auto p = __builtin_amdgcn_permlane16_swap(v[0], v[2], false, false);
  const auto q = __builtin_amdgcn_permlane16_swap(v[1], v[3], false, false);
  return u32x4{p[0], q[0], p[1], q[1]};
}
__device__ __forceinline__ unsigned pack2(float a, float b) {
  f32x2 f = {a, b};
  return __builtin_bit_cast(unsigned, __builtin_convertvector(f, bf16x2));
}
__device__ __forceinline__ float bflo(unsigned u) { return __builtin_bit_cast(float, u << 16); }
__device__ __forceinline__ float bfhi(unsigned u) { return __builtin_bit_cast(float, u & 0xffff0000u); }
__device__ __forceinline__ float asf(unsigned u) { return __builtin_bit_cast(float, u); }
__device__ __forceinline__ unsigned asu(float f) { return __builtin_bit_cast(unsigned, f); }
__device__ __forceinline__ f32x2 pk_fma(f32x2 a, f32x2 b, f32x2 c) { return __builtin_elementwise_fma(a, b, c); }
__device__ __forceinline__ f32x2 splat(float v) { return f32x2{v, v}; }
__device__ __forceinline__ f32x2 half2(const f32x4& v, int h) { return f32x2{v[2 * h], v[2 * h + 1]}; }
#define ROR1(R, N) "v_add_f32_dpp " R ", " R ", " R " row_ror:" #N " row_mask:0xf bank_mask:0xf\n\t"
__device__ __forceinline__ float sum_half32(float v) {
  asm volatile("s_nop 1\n\t" ROR1("%0", 8) "s_nop 1\n\t" ROR1("%0", 4) "s_nop 1\n\t" ROR1("%0", 2) "s_nop 1\n\t" ROR1("%0", 1) : "+v"(v));
  float t = v;
  asm volatile("s_nop 1\n\tv_permlane16_swap_b32 %0, %1\n\ts_nop 1" : "+v"(v), "+v"(t));
  return v + t;
}
__device__ __forceinline__ u64 mix64(u64 z) {
  z += 0x9E3779B97F4A7C15ull;
  z = (z ^ (z >> 30)) * 0xBF58476D1CE4E5B9ull;
  z = (z ^ (z >> 27)) * 0x94D049BB133111EBull;
  return z ^ (z >> 31);
}

__device__ __forceinline__ void word_set(int word, unsigned v) { CFENCE(); *(volatile LAS unsigned*)(M_SY + word * 4) = v; CFENCE(); }
template <int SL = 0> __device__ __forceinline__ void word_wait(int word) {
#pragma unroll 1
  for (unsigned spins = 0; spins < SPIN_CAP * 8; ++spins) {
    if (__builtin_amdgcn_readfirstlane(*(volatile LAS unsigned*)(M_SY + word * 4))) break;
    if (SL) __builtin_amdgcn_s_sleep(SL);
  }
  CFENCE();
}
__device__ __forceinline__ unsigned min4(u32x4 v) { return __builtin_amdgcn_readfirstlane(min(min(v.x, v.y), min(v.z, v.w))); }
template <int SL = 0> __device__ __forceinline__ void lvl_wait(int word, unsigned v) {
#pragma unroll 1
  for (unsigned spins = 0; spins < SPIN_CAP * 8; ++spins) {
    if (min4(*(volatile LAS u32x4*)(M_SY + word * 4)) >= v) break;
    if (SL) __builtin_amdgcn_s_sleep(SL);
  }
  CFENCE();
}
template <int N> __device__ __forceinline__ unsigned pub_arrive(int word) {
  unsigned old;
  asm volatile("s_waitcnt vmcnt(%3)\n\ts_mov_b64 exec, 1\n\tds_add_rtn_u32 %0, %1, %2\n\ts_mov_b64 exec, -1" : "=&v"(old) : "v"(M_SY + word * 4), "v"(1u), "i"(N) : "memory");
  return old;
}
__device__ __forceinline__ void pub_finish(unsigned old, char* flag, u64 tag) {
  unsigned t;
  asm volatile("s_waitcnt lgkmcnt(0)\n\tv_readfirstlane_b32 %0, %1\n\ts_cmp_lg_u32 %0, 3\n\ts_cbranch_scc1 1f\n\ts_mov_b64 exec, 1\n\t"
               "global_store_dwordx2 %2, %3, %4 sc1\n\ts_mov_b64 exec, -1\n1:" : "=&s"(t) : "v"(old), "v"(0u), "v"(tag), "s"(flag) : "memory", "scc");
}

__device__ __forceinline__ constexpr int tile_src(int p) {
  return p < 8 ? 7 * (p >> 1) + 2 * (p & 1) : p < 20 ? 7 * ((p - 8) / 3) + ((p - 8) % 3 == 2 ? 1 : 4 + 2 * ((p - 8) % 3)) : 7 * ((p - 20) >> 1) + 3 + 2 * ((p - 20) & 1);
}

template <int NB> struct Frags { u32x4 x[4]; u32x2 wl[NB], wh[NB]; };
template <bool HT, bool RD, class Hook>
__device__ __forceinline__ void kstep(const Frags<HT ? 7 : 6>& fc, Frags<HT ? 7 : 6>& fn, unsigned xa, unsigned wa, unsigned wb, f32x4 (&acc)[7][4], Hook&& hook) {
  constexpr int NB = HT ? 7 : 6, NA = HT ? 4 : 3;
#pragma unroll
  for (int i = 0; i < 4 * NB; ++i) {
    const int b = i >> 2, rb = i & 3;
    const bool sw = !HT && (b % 3) != 0;
    const u32x4 wf = {fc.wl[b].x, fc.wl[b].y, fc.wh[b].x, fc.wh[b].y};
    acc[b][rb] = sw ? mfma32(fc.x[rb], wf, acc[b][rb]) : mfma32(wf, fc.x[rb], acc[b][rb]);
    SB();
    if (RD) {
      if (i < 4) fn.x[i] = *(LAS u32x4*)(xa + i * 2048);
      else if (i < 4 + 2 * NB) {
        const int q = (i - 4) >> 1;
        const unsigned a = q < NA ? wa + q * 8192 : wb + (q - NA) * 8192;
        if (i & 1) fn.wh[q] = trread(a + 512); else fn.wl[q] = trread(a);
      }
    }
    hook(i);
    SB();
  }
}
template <bool HT, class Tail>
__device__ __forceinline__ void g1_compute(unsigned xbase, unsigned wbA, unsigned wbB, int hw, f32x4 (&acc)[7][4], Tail&& tail) {
  constexpr int NB = HT ? 7 : 6, NA = HT ? 4 : 3;
  Frags<NB> f0, f1;
#pragma unroll
  for (int b = 0; b < NB; ++b)
#pragma unroll
    for (int rb = 0; rb < 4; ++rb) acc[b][rb] = f32x4{0, 0, 0, 0};
  asm volatile("" : "+v"(xbase), "+v"(wbA), "+v"(wbB));
  asm volatile("s_waitcnt lgkmcnt(0)\n\ts_barrier" ::: "memory");
#pragma unroll
  for (int i = 0; i < 4; ++i) f0.x[i] = *(LAS u32x4*)(xbase + i * 2048);
#pragma unroll
  for (int q = 0; q < NB; ++q) { const unsigned a = q < NA ? wbA + q * 8192 : wbB + (q - NA) * 8192; f0.wl[q] = trread(a); f0.wh[q] = trread(a + 512); }
  kstep<HT, true>(f0, f1, xbase + 512, wbA + 4096, wbB + 4096, acc, [](int) {});
  int slot = 0;
#pragma unroll 1
  for (int ci = 0; ci < 7; ++ci) {
    slot += STAGE; if (slot == 3 * STAGE) slot = 0;
    unsigned xn = xbase + slot, wan = wbA + slot, wbn = wbB + slot;
    asm volatile("" : "+v"(xn), "+v"(wan), "+v"(wbn));
    LGKM0();
    asm volatile("s_barrier" ::: "memory");
    kstep<HT, true>(f1, f0, xn, wan, wbn, acc, [](int) {});
    kstep<HT, true>(f0, f1, xn + 512, wan + 4096, wbn + 4096, acc, [](int) {});
  }
  kstep<HT, false>(f1, f0, 0, 0, 0, acc, [&](int i) {
    if (i == 0) word_set(SY_DONE + hw, 1u);
    tail(i);
  });
}

template <int N> __device__ __forceinline__ void phi_pack(f32x2 (&t)[N], unsigned (&o)[N]) {
  const f32x2 L = splat(1.4426950408889634f), nL = splat(-1.4426950408889634e-6f);
  f32x2 e[N];
#pragma unroll
  for (int i = 0; i < N; ++i) e[i] = pk_fma(t[i], L, nL);
  SB();
#pragma unroll
  for (int i = 0; i < N; ++i)
    e[i] = f32x2{__builtin_amdgcn_fmed3f(__builtin_amdgcn_exp2f(e[i][0]), 0.0f, 1.0f), __builtin_amdgcn_fmed3f(__builtin_amdgcn_exp2f(e[i][1]), 0.0f, 1.0f)};
  SB();
#pragma unroll
  for (int i = 0; i < N; ++i) { asm("v_max_f32 %0, 0x358637bd, %0" : "+v"(t[i][0])); asm("v_max_f32 %0, 0x358637bd, %0" : "+v"(t[i][1])); }
  SB();
#pragma unroll
  for (int i = 0; i < N; ++i) t[i] = t[i] + e[i];
  SB();
#pragma unroll
  for (int i = 0; i < N; ++i) o[i] = pack2(t[i][0], t[i][1]);
  SB();
}

__global__ __launch_bounds__(512, 2) void fused_block_kernel(const char* __restrict__ x, const char* __restrict__ wqkv, const char* __restrict__ wo,
                                                            const char* __restrict__ w1, const char* __restrict__ b1, const char* __restrict__ w2,
                                                            const char* __restrict__ b2, char* __restrict__ out, char* __restrict__ ws) {
  extern __shared__ __attribute__((aligned(16))) char smem[];
  const int tid = threadIdx.x, lane = tid & 63, w = __builtin_amdgcn_readfirstlane(tid >> 6), hw = w & 3;
  const int bid = blockIdx.x, xcd = bid & 7, r = bid >> 3;
  {
    u64 pc; unsigned len;
    asm volatile("s_getpc_b64 %0\n1:\n\ts_mov_b32 %1, .Lfused_block_tail-1b\n\ts_mov_b32 m0, %2" : "=s"(pc), "=s"(len) : "i"(M_DUMP));
    asm volatile("global_load_lds_dword %0, %1" :: "v"(min((unsigned)(w * 32 + r) * 128u, len)), "s"(pc) : "memory");
  }
  const int rp = (xcd >> 2) * 8 + (r >> 2), cg = (xcd & 3) * 4 + (r & 3);
  const int j = cg >> 1;
  const bool ht = cg & 1;
  const int hoff = (j * 448) & ~127;
  const int wstride = ht ? 3584 : 3072, c16 = lane & 7;
  const unsigned wsw = (((c16 >> 1) ^ ((lane >> 4) & 1)) << 5) | ((c16 & 1) << 4);
  unsigned gv[12];
  {
    const unsigned xv = (rp * 128 + hw * 32 + ((lane >> 2) & 7)) * 1024 + ((lane >> 5) * 4 + (lane & 3)) * 16;
#pragma unroll
    for (int i = 0; i < 4; ++i) gv[i] = (xv ^ ((i & 1) * 32)) + i * (8192 - 1024);
    const unsigned wv = (lane >> 3) * wstride + (ht ? hoff : j * 128) + wsw + 4096;
#pragma unroll
    for (int i = 0; i < 8; ++i) {
      const int p = (ht ? 8 : 6) * hw + i;
      gv[4 + i] = (wv ^ ((i & 1) * 64)) + ((p & 7) * 8 * wstride + (p >> 3) * (ht ? 128 : 1024) - i * 1024);
    }
  }
  unsigned sv = (rp * 128 + cg * 8 + hw * 2 + (lane >> 5)) * 1024 + (lane & 31) * 16;
  const int cu = bid % 52, crb = bid < 208 ? bid / 52 : 0, cst = cu < 24 ? 3072 : 3584;
  if (w < 4) {
    const unsigned cv = (crb * 128 + hw * 32 + (lane >> 3)) * cst + (cu < 24 ? cu : cu - 24) * 128 + wsw;
#pragma unroll
    for (int i = 0; i < 4; ++i) gv[i] = (cv ^ ((i & 1) * 64)) + i * (8 * cst - 1024);
  }
#pragma unroll
  for (int i = 0; i < 12; i += 4) asm volatile("" : "+v"(gv[i]), "+v"(gv[i + 1]), "+v"(gv[i + 2]), "+v"(gv[i + 3]));
  asm volatile("" : "+v"(sv));
  SB();
  asm volatile("" : "+s"(x), "+s"(wqkv), "+s"(wo), "+s"(w1), "+s"(b1), "+s"(w2), "+s"(b2), "+s"(out), "+s"(ws));
  u64 epoch = *(const __attribute__((address_space(4))) u64*)(ws + WS_EPOCH);
  const unsigned htm = ht ? 1u : 0u;
  u32x4 xa, xb, sa = {0u, 0u, 0u, 0u}, sb = sa, sc = sa;
  const char* const pst = ws + WS_STAT + (rp * 128 + hw * 32) * 16;
  const char* const pcs = ws + WS_CS + (ht ? 24 + hoff / 128 + hw : (hw < 3 ? hw : 0) * 8 + j) * 2048;
  auto side_req = [&]() {
    asm volatile("global_load_dwordx4 %0, %3, %5 sc1\n\tglobal_load_dwordx4 %1, %4, %6 sc1\n\tglobal_load_dwordx4 %2, %4, %6 offset:1024 sc1"
                 : "+v"(sa), "+v"(sb), "+v"(sc) : "v"((lane & 31) * 16), "v"(lane * 16), "s"(pst), "s"(pcs) : "memory");
  };
  if (w >= 4) {
    const char* px = x;
    const char* pw = ht ? w1 : wqkv;
    const char *tb1, *tb2;
    auto chunk = [&](int so, bool first) {
#define GL(V, P, O) "global_load_lds_dwordx4 %" #V ", %" #P " offset:" #O "\n\t"
      asm volatile("s_mov_b32 m0, %0\n\ts_nop 0\n\t" GL(2, 8, 0) GL(3, 8, 1024) GL(4, 8, 2048) GL(5, 8, 3072) "s_mov_b32 m0, %1\n\ts_nop 0\n\t" GL(6, 9, -4096) GL(7, 9, -3072)
                   :: "s"(so + hw * 4096), "s"(so + 20480 + hw * (ht ? 8192 : 6144)), "v"(gv[0]), "v"(gv[1]), "v"(gv[2]), "v"(gv[3]), "v"(gv[4]), "v"(gv[5]), "s"(px), "s"(pw) : "memory");
      if (first) {
        const int l = lane < 18 ? lane : 0, L = (hw * 18 + l) * 256 + bid, r1 = L >> 3, r2 = r * 72 + hw * 18 + l;
        tb1 = (r1 < 1792 ? w2 + r1 * 1024 : wo + (r1 - 1792) * 1024) + (L & 7) * 128;
        tb2 = (r2 < 1792 ? w2 + r2 * 1024 : wo + (r2 - 1792) * 1024) + 384;
        asm volatile("s_waitcnt vmcnt(6)\n\ts_barrier" : "+v"(tb1), "+v"(tb2) :: "memory");
      }
      asm volatile(GL(0, 6, -2048) GL(1, 6, -1024) GL(2, 6, 0) GL(3, 6, 1024) "s_cmp_eq_u32 %7, 0\n\ts_cbranch_scc1 1f\n\t" GL(4, 6, 2048) GL(5, 6, 3072) "\n1:"
                   :: "v"(gv[6]), "v"(gv[7]), "v"(gv[8]), "v"(gv[9]), "v"(gv[10]), "v"(gv[11]), "s"(pw), "s"(htm) : "memory", "scc");
      px += 128; pw += 64 * wstride;
    };
    chunk(0, false);
    chunk(STAGE, true);
    if (hw == 0) glds16(M_B1, (lane & 31) * 16, b1 + hoff);
    int fs = 2 * STAGE;
#pragma unroll 1
    for (int k = 0; k < 5; ++k) {
      chunk(fs, false);
      fs += STAGE; if (fs == 3 * STAGE) fs = 0;
      asm volatile("s_cmp_lg_u32 %0, 0\n\ts_cbranch_scc1 1f\n\ts_waitcnt vmcnt(10)\n1:\n\ts_waitcnt vmcnt(12)\n\ts_barrier" :: "s"(htm) : "memory", "scc");
    }
    side_req();
    chunk(fs, false);
    touch(tb1); touch(tb2);
    asm volatile("s_cmp_lg_u32 %3, 0\n\ts_cbranch_scc1 1f\n\ts_waitcnt vmcnt(%4)\n1:\n\ts_waitcnt vmcnt(%5)\n\ts_barrier"
                 : "+v"(sa), "+v"(sb), "+v"(sc) : "s"(htm), "i"(12), "i"(14) : "memory", "scc");
  } else {
    asm volatile("global_load_dwordx4 %0, %2, %3\n\tglobal_load_dwordx4 %1, %2, %3 offset:512" : "=&v"(xa), "=&v"(xb) : "v"(sv), "s"(x) : "memory");
    asm volatile("s_mov_b32 m0, %0\n\ts_nop 0\n\t" GL(1, 5, 0) GL(2, 5, 1024) GL(3, 5, 2048) GL(4, 5, 3072)
                 :: "s"(2 * STAGE + hw * 4096), "v"(gv[0]), "v"(gv[1]), "v"(gv[2]), "v"(gv[3]), "s"(cu < 24 ? wqkv : w1) : "memory");
  }
  int w_ = w;
  asm volatile("" : "+s"(w_));
  const int r2 = r ^ (((0x2100 >> r) & 1) * 5) ^ (((0x48000 >> r) & 1) * 29);
  const int rt = xcd * 4 + ((r2 & 3) ^ ((r2 >> 2) & 1)), ct = r2 >> 2;
  const int g = lane >> 4, m = lane & 15, q4 = m >> 2, p4 = lane & 3;

  if (w_ >= 4) {
    asm volatile("" : "+s"(epoch));
    const u64 tag = mix64(epoch);
    const unsigned tag32 = (unsigned)tag;
    const unsigned bv = (lane >> 3) * 1024 + ct * 128 + ((c16 ^ (((lane >> 4) & 1) << 2)) << 4);
    auto pace = [&]() { asm volatile("s_cmp_eq_u32 %0, 0\n\ts_cbranch_scc1 1f\n\ts_waitcnt vmcnt(%1)\n1:\n\ts_waitcnt vmcnt(%2)" :: "s"(htm), "i"(PACE_H), "i"(PACE_Q) : "memory", "scc"); };
    auto bstage = [&](int t, int so) __attribute__((always_inline)) {
#pragma unroll
      for (int i = 0; i < 3; ++i) {
        const int sr = (hw & 1) ? tile_src(6 * t + 3 + i) : tile_src(6 * t + i);
        const char* q = w2 + ((hw >> 1) * 28 + sr) * 32768;
        if (t == 2) pace();
        asm volatile("s_mov_b32 m0, %0\n\ts_nop 0\n\t" GL(1, 5, 0) GL(2, 5, 1024) GL(3, 5, 2048) GL(4, 5, 3072)
                     :: "s"(so + hw * 12288 + i * 4096), "v"(bv), "v"(bv + 7168), "v"(bv + 2 * 7168), "v"(bv + 3 * 7168), "s"(q) : "memory");
      }
    };
#pragma unroll 1
    for (unsigned spins = 0; spins < SPIN_CAP && __any(((sa.y ^ tag32) | (sa.w ^ tag32) | (sb.y ^ tag32) | (sb.w ^ tag32) | (sc.y ^ tag32) | (sc.w ^ tag32)) != 0u); ++spins) {
      side_req();
      asm volatile("s_waitcnt vmcnt(0)" : "+v"(sa), "+v"(sb), "+v"(sc) :: "memory");
    }
    {
      float c0 = asf(sb.x) + asf(sc.x), c1 = asf(sb.z) + asf(sc.z), d0 = c0, d1 = c1;
      asm volatile("s_nop 1\n\tv_permlane32_swap_b32 %0, %1\n\tv_permlane32_swap_b32 %2, %3\n\ts_nop 1" : "+v"(c0), "+v"(d0), "+v"(c1), "+v"(d1));
      const unsigned l4 = (hw * 32 + (lane & 31)) * 4;
      *(LAS unsigned*)(M_MR + l4) = sa.x; *(LAS unsigned*)(M_RSTD + l4) = sa.z;
      *(LAS f32x2*)(M_CS + 2 * l4) = f32x2{c0 + d0, c1 + d1};
    }
    asm volatile("s_waitcnt vmcnt(2) lgkmcnt(0)\n\ts_barrier" ::: "memory");
    bstage(0, 2 * STAGE);
    bstage(1, 0);
    lvl_wait<1>(SY_DONE, 1u);
    asm volatile("s_waitcnt vmcnt(12)" ::: "memory");
    word_set(SY_LAND + hw, 1u);
    bstage(2, STAGE);
    if (hw < 3) {
      word_set(SY_LVL + hw, 1u);
      u32x4 braw[48];
      const unsigned bv2 = ((lane >> 5) * 8 + ((lane >> 3) & 3)) * 1024 + ct * 128 + (((lane & 7) ^ ((lane & 1) << 2)) << 4);
#pragma unroll
      for (int i = 0; i < 6; ++i) {
        const int o0 = 3 * i < 10 ? tile_src(18 + 3 * i) : 3 * i - 10, o1 = 3 * i + 1 < 10 ? tile_src(19 + 3 * i) : 3 * i - 9, o2 = 3 * i + 2 < 10 ? tile_src(20 + 3 * i) : 3 * i - 8;
        const bool fromo = 3 * i + hw >= 10;
        const char* q = (fromo ? wo : w2) + (hw == 0 ? o0 : hw == 1 ? o1 : o2) * 32768;
        const int khs = fromo ? 8 * 32768 : 28 * 32768;
#pragma unroll
        for (int e = 0; e < 8; e += 4) {
          pace();
          if (i < 2)
            asm volatile("global_load_dwordx4 %0, %4, %8\n\tglobal_load_dwordx4 %1, %5, %8 offset:1024\n\tglobal_load_dwordx4 %2, %6, %8 offset:2048\n\tglobal_load_dwordx4 %3, %7, %8 offset:3072"
                         : "=&v"(braw[i * 8 + e]), "=&v"(braw[i * 8 + e + 1]), "=&v"(braw[i * 8 + e + 2]), "=&v"(braw[i * 8 + e + 3])
                         : "v"(bv), "v"(bv + 7168), "v"(bv + 2 * 7168), "v"(bv + 3 * 7168), "s"(q + (e >> 2) * khs) : "memory");
          else
            asm volatile("global_load_dwordx4 %0, %4, %6 offset:-2048\n\tglobal_load_dwordx4 %1, %4, %6 offset:2048\n\tglobal_load_dwordx4 %2, %5, %6 offset:-2048\n\tglobal_load_dwordx4 %3, %5, %6 offset:2048"
                         : "=&v"(braw[i * 8 + e]), "=&v"(braw[i * 8 + e + 1]), "=&v"(braw[i * 8 + e + 2]), "=&v"(braw[i * 8 + e + 3])
                         : "v"(bv2 + 2048), "v"(bv2 + 2048 + 16384), "s"(q + (e >> 2) * khs) : "memory");
        }
        if (i == 1) { static_assert(PACE_H <= 12 && PACE_Q <= 12, "the first 36 tiles have landed here"); word_set(SY_LVL + hw, 3u); }
      }
      const unsigned bwr = hw * 4096 + lane * 16;
      auto put = [&](int i) {
        constexpr int so[3] = {2 * STAGE, 0, STAGE};
#pragma unroll
        for (int e = 0; e < 8; ++e) {
          if (i < 2) *(LAS u32x4*)(bwr + so[i >> 1] + (i & 1) * 12288 + (e >> 2) * 24576 + (e & 3) * 1024) = braw[i * 8 + e];
          else {
            const u32x4 v = braw[i * 8 + e];
            asm volatile("s_mov_b32 m0, %4\n\ts_nop 0\n\tds_write_addtid_b32 %0 offset:%5\n\tds_write_addtid_b32 %1 offset:%5+256\n\t"
                         "ds_write_addtid_b32 %3 offset:%5+1024\n\tds_write_addtid_b32 %2 offset:%5+1280"
                         :: "v"(v.x), "v"(v.y), "v"(v.z), "v"(v.w), "s"(so[i >> 1] + hw * 4096), "i"((i & 1) * 12288 + (e >> 2) * 24576 + (e & 2) * 1024 + (e & 1) * 512) : "memory");
          }
        }
      };
      auto shuffle = [&](int i) {
#pragma unroll
        for (int n = 8 * i; n < 8 * i + 8; n += 2) {
          unsigned t[8];
#define CND(D, A, B) "v_cndmask_b32_dpp %" #D ", %" #A ", %" #B ", vcc quad_perm:[1,0,3,2] row_mask:0xf bank_mask:0xf\n\t"
          asm volatile("s_mov_b64 vcc, %16\n\ts_nop 0\n\t" CND(2, 11, 10) CND(6, 15, 14) CND(0, 9, 8) CND(4, 13, 12)
                       "s_not_b64 vcc, vcc\n\t" CND(3, 10, 11) CND(7, 14, 15) CND(1, 8, 9)
                       "v_permlane32_swap_b32 %2, %3\n\t" CND(5, 12, 13) "v_permlane32_swap_b32 %6, %7\n\t"
                       "v_permlane32_swap_b32 %3, %2\n\ts_nop 0\n\tv_permlane32_swap_b32 %7, %6"
                       : "=&v"(t[0]), "=&v"(t[1]), "=&v"(t[2]), "=&v"(t[3]), "=&v"(t[4]), "=&v"(t[5]), "=&v"(t[6]), "=&v"(t[7])
                       : "v"(braw[n].x), "v"(braw[n].y), "v"(braw[n].z), "v"(braw[n].w), "v"(braw[n + 1].x), "v"(braw[n + 1].y), "v"(braw[n + 1].z), "v"(braw[n + 1].w),
                         "s"(0x5555555555555555ull) : "vcc", "scc");
          braw[n] = u32x4{t[0], t[1], t[2], t[3]}; braw[n + 1] = u32x4{t[4], t[5], t[6], t[7]};
        }
      };
      auto landed = [&](int n0, int n1) {
#pragma unroll
        for (int n = n0; n < n1; n += 4) asm volatile("" : "+v"(braw[n]), "+v"(braw[n + 1]), "+v"(braw[n + 2]), "+v"(braw[n + 3]));
      };
      asm volatile("s_waitcnt vmcnt(16)" ::: "memory");
      landed(0, 32);
      lvl_wait<2>(SY_CONS, 1u); put(0); word_set(SY_LVL + hw, 4u); put(1);
      shuffle(2); shuffle(3);
      asm volatile("s_waitcnt vmcnt(0)" ::: "memory");
      landed(32, 48);
      shuffle(4); shuffle(5);
      lvl_wait<1>(SY_CONS, 3u); put(2); word_set(SY_LVL + hw, 5u);
      lvl_wait<1>(SY_CONS, 4u); put(3); put(4); word_set(SY_LVL + hw, 6u);
      lvl_wait<1>(SY_CONS, 5u); put(5); word_set(SY_LVL + hw, 7u);
    } else {
      const char* fl = ws + WS_FLAG + (rt >> 1) * 32 * FS;
      word_wait<2>(ht ? SY_CNT_A : SY_CONS);
      unsigned spins = 0;
#pragma unroll 1
      for (int bt = 0; bt < 4; ++bt) {
#pragma unroll 1
        for (; spins < SPIN_CAP; ++spins) {
          u64 fv;
          asm volatile("global_load_dwordx2 %0, %1, %2 sc1\n\ts_waitcnt vmcnt(0)" : "=&v"(fv) : "v"((lane & 7) * FS), "s"(fl + bt * 8 * FS) : "memory");
          if (__all(fv == tag)) break;
          __builtin_amdgcn_s_sleep(4);
        }
        word_set(SY_LVL + 3, bt == 0 ? 1u : bt == 1 ? 3u : bt == 2 ? 4u : 7u);
      }
      if (bid == 0) {
        GAS u64* p = (GAS u64*)(ws + WS_FLAG + ((lane >> 4) * 32 + ((lane >> 3) & 1) * 24 + (lane & 7)) * FS);
#pragma unroll 1
        for (spins = 0; spins < SPIN_CAP; ++spins) {
          u64 v0 = __hip_atomic_load(p, RLX_AGENT), v1 = __hip_atomic_load(p + 16 * FS, RLX_AGENT), v2 = __hip_atomic_load(p + 32 * FS, RLX_AGENT), v3 = __hip_atomic_load(p + 48 * FS, RLX_AGENT);
          if (__all(v0 == tag && v1 == tag && v2 == tag && v3 == tag)) break;
          __builtin_amdgcn_s_sleep(1);
        }
        if (lane == 0) __hip_atomic_store((GAS u64*)(ws + WS_EPOCH), tag, RLX_AGENT);
      }
    }
    asm volatile(".Lfused_block_tail:\n\ts_nop 0" ::: "memory");
  } else {
    __builtin_amdgcn_s_setprio(2);
    const __amdgpu_buffer_rsrc_t rs = __builtin_amdgcn_make_buffer_rsrc(ws, 0, WS_END, 0x00020000);
    const int fsw = (q4 >> 1) | ((g & 1) << 1);
    const unsigned wb0 = g * 1024 + q4 * 128 + 8 * p4;
    unsigned wrm = 0, tag32;
    u64 tag;
    {
      touch((hw == 0 && (xcd & 3) == 1) ? wqkv + ((lane < 48 ? lane : 0) * 32 + r) * 1024 + 384 : x);
      asm volatile("global_load_dword %0, %1, %2 sc1" : "+v"(wrm)
                   : "v"(lane >= 32 && !ht ? WS_KVP + (j * 64 + hw * 16 + m) * KVREC + 1024 : WS_FLAG + ((rt >> 1) * 32 + hw * 8 + (lane & 7)) * FS), "s"(ws) : "memory");
      if (tid < 64) *(LAS unsigned*)(M_SY + tid * 4) = 0u;
      asm volatile("" : "+s"(epoch));
      tag = mix64(epoch);
      tag32 = (unsigned)tag;
      asm volatile("s_waitcnt vmcnt(6)" : "+v"(xa), "+v"(xb) :: "memory");
      float v[16], s1 = 0.f, s2 = 0.f;
#pragma unroll
      for (int i = 0; i < 4; ++i) { v[2 * i] = bflo(xa[i]); v[2 * i + 1] = bfhi(xa[i]); v[8 + 2 * i] = bflo(xb[i]); v[9 + 2 * i] = bfhi(xb[i]); }
#pragma unroll
      for (int i = 0; i < 16; ++i) s1 += v[i];
      const float mean = sum_half32(s1) * (1.0f / 512.0f);
#pragma unroll
      for (int i = 0; i < 16; ++i) s2 = __builtin_fmaf(v[i] - mean, v[i] - mean, s2);
      float rstd = __builtin_amdgcn_rsqf(sum_half32(s2) * (1.0f / 512.0f) + 1e-5f);
      asm volatile("s_waitcnt vmcnt(2)" : "+v"(rstd) :: "memory");
      word_set(SY_PRE + hw, tag32);
      if ((lane & 31) == 0)
        __builtin_amdgcn_raw_buffer_store_b128(u32x4{asu(-mean * rstd), tag32, asu(rstd), tag32}, rs, WS_STAT + (sv >> 10) * 16, 0, 16);
#pragma unroll 1
      for (unsigned spins = 0; spins < SPIN_CAP * 8; ++spins) {
        const u32x4 t = *(volatile LAS u32x4*)(M_SY + SY_PRE * 4);
        if (__builtin_amdgcn_readfirstlane((t.x ^ tag32) | (t.y ^ tag32) | (t.z ^ tag32) | (t.w ^ tag32)) == 0u) break;
      }
      CFENCE();
      const u32x4 ones = {0x3F803F80u, 0x3F803F80u, 0x3F803F80u, 0x3F803F80u};
      f32x4 cs = {0.f, 0.f, 0.f, 0.f};
#pragma unroll
      for (int s = 0; s < 4; ++s) {
        const unsigned a = 2 * STAGE + s * 4096 + wb0 + ((hw ^ fsw) << 5);
        const u32x2 lo = trread(a), hi = trread(a + 512);
        cs = mfma32(ones, u32x4{lo.x, lo.y, hi.x, hi.y}, cs);
      }
      asm volatile("" : "+v"(cs));
      if (g == 0 && bid < 208) __builtin_amdgcn_raw_buffer_store_b64(u32x2{asu(cs[0]), tag32}, rs, WS_CS + ((cu * 4 + crb) * 64 + hw * 16 + m) * 8, 0, 16);
    }
    const int rg = hw >> 1, ch = hw & 1;
    const bool jo = ht && (j & 1);
    const int cbA = (ht ? jo : rg) ? ch + 2 : ch, cbB = cbA ^ 2, u0B = jo ? 1 : 0;
    const unsigned xbase = rg * 8192 + (m >> 3) * 1024 + (m & 7) * 64 + ((g ^ ((m >> 3) << 1)) << 4);
    const unsigned wbA = 16384 + wb0 + ((cbA ^ fsw) << 5), wbB = 16384 + wb0 + u0B * 8192 + ((cbB ^ fsw) << 5);
    const int P0 = rp * 4 + rg * 2;
    char* const flags = ws + WS_FLAG + (rp * 32 + j) * FS;
    const int kh = hw >> 1, rh = hw & 1;
    const unsigned ooff = (rt * 64 + rh * 32 + (lane & 31)) * 1024 + ct * 128 + kh * 64 + (lane >> 5) * 16;
    u32x4 xres[2], b2v[2];
    auto load_res = [&]() {
      asm volatile("global_load_dwordx4 %0, %4, %6\n\tglobal_load_dwordx4 %1, %4, %6 offset:32\n\tglobal_load_dwordx4 %2, %5, %7\n\tglobal_load_dwordx4 %3, %5, %7 offset:32"
                   : "=&v"(xres[0]), "=&v"(xres[1]), "=&v"(b2v[0]), "=&v"(b2v[1]) : "v"(ooff), "v"(ooff & 1023), "s"(x), "s"(b2) : "memory");
    };
    u32x4 kt[17];
    auto kreq = [&](unsigned mask) {
      const char* rec = ws + WS_KVP + (j * 4 + cbA) * 16 * KVREC;
      const char* rec0 = rec + (rp & 8) * KVREC;
      const char* rec1 = rec + ((rp & 8) ^ 8) * KVREC;
      const unsigned zo = m * KVREC + 1024 + g * 16;
#pragma unroll
      for (int i = 0; i < 17; ++i)
        asm volatile("s_bitcmp0_b32 %1, %4\n\ts_cbranch_scc1 1f\n\tglobal_load_dwordx4 %0, %2, %3 sc1\n1:" : "+v"(kt[i])
                     : "s"(mask), "v"(i < 16 ? lane * 16 : zo), "s"(i < 8 ? rec0 + i * KVREC : i < 16 ? rec1 + (i - 8) * KVREC : rec), "i"(i) : "memory", "scc");
    };
    f32x4 acc[7][4];
    unsigned pq[2][8];
    if (ht) {
      float rstd[4], mr[4];
      u32x2 bi[7];
      f32x4 cf[7];
      unsigned am, aA, aB;
      g1_compute<true>(xbase, wbA, wbB, hw, acc, [&](int i) {
        if (i == 1) { am = M_RSTD + (rg * 64 + m) * 4; asm volatile("" : "+v"(am)); }
        if (i == 2) { aA = cbA * 32 + g * 8; asm volatile("" : "+v"(aA)); }
        if (i == 3) { aB = cbB * 32 + u0B * 128 + g * 8; asm volatile("" : "+v"(aB)); }
        if (i >= 4 && i < 8) { rstd[i - 4] = *(LAS float*)(am + (i - 4) * 64); mr[i - 4] = *(LAS float*)(am + (i - 4) * 64 + (M_MR - M_RSTD)); }
        if (i >= 8 && i < 22) {
          const int b = (i - 8) >> 1;
          const unsigned o = b < 4 ? aA + b * 128 : aB + (b - 4) * 128;
          if (i & 1) bi[b] = *(LAS u32x2*)(M_B1 + o); else cf[b] = *(LAS f32x4*)(M_CS + 2 * o);
        }
      });
      const unsigned sbase = lane * 16 + WS_A + (P0 * 144 + (j >> 2) * 72 + ch) * 1024;
      const f32x2 k0 = splat(-2.3022081985378894f), k1 = splat(-2.3022081985378894f * 0.044715f), one = splat(1.0f);
      u32x4 pend; unsigned pendo, old1;
#pragma unroll
      for (int n = 0; n < 14; ++n) {
        const int b = n >> 1, pr = n & 1;
        const int pos = b < 2 ? (j & 3) * 4 + 2 * b : b < 5 ? 16 + (j & 3) * 6 + 2 * (b - 2) : 40 + (j & 3) * 4 + 2 * (b - 5);
        const f32x2 bb[2] = {f32x2{bflo(bi[b].x), bfhi(bi[b].x)}, f32x2{bflo(bi[b].y), bfhi(bi[b].y)}};
        f32x2 u[4], t[4];
#pragma unroll
        for (int i = 0; i < 4; ++i) u[i] = pk_fma(splat(mr[2 * pr + (i >> 1)]), half2(cf[b], i & 1), bb[i & 1]);
        if (n > 0) pend = to_frag32(pend);
        SB();
#pragma unroll
        for (int i = 0; i < 4; ++i) u[i] = pk_fma(half2(acc[b][2 * pr + (i >> 1)], i & 1), splat(rstd[2 * pr + (i >> 1)]), u[i]);
        SB();
        if (n > 0) __builtin_amdgcn_raw_buffer_store_b128(pend, rs, pendo, 0, 16);
        if (n == 2) load_res();
        if (n == 9) old1 = pub_arrive<5>(SY_CNT_A);
#pragma unroll
        for (int i = 0; i < 4; ++i) t[i] = u[i] * u[i];
        SB();
#pragma unroll
        for (int i = 0; i < 4; ++i) t[i] = pk_fma(t[i], k1, k0);
        SB();
#pragma unroll
        for (int i = 0; i < 4; ++i) t[i] = t[i] * u[i];
        SB();
#pragma unroll
        for (int i = 0; i < 4; ++i) t[i] = f32x2{__builtin_amdgcn_exp2f(t[i][0]), __builtin_amdgcn_exp2f(t[i][1])};
        if (n == 9) {
#pragma unroll
          for (int i = 0; i < 4; ++i) asm volatile("" : "+v"(t[i]));
          pub_finish(old1, flags, tag);
        }
        SB();
#pragma unroll
        for (int i = 0; i < 4; ++i) t[i] = t[i] + one;
        SB();
#pragma unroll
        for (int i = 0; i < 4; ++i) t[i] = f32x2{__builtin_amdgcn_rcpf(t[i][0]), __builtin_amdgcn_rcpf(t[i][1])};
        SB();
#pragma unroll
        for (int i = 0; i < 4; ++i) t[i] = t[i] * u[i];
        SB();
        pend = u32x4{pack2(t[0][0], t[0][1]), pack2(t[1][0], t[1][1]), pack2(t[2][0], t[2][1]), pack2(t[3][0], t[3][1])};
        asm volatile("" : "+v"(pend));
        pendo = sbase + (pr * 144 + pos) * 1024;
        SB();
      }
      __builtin_amdgcn_raw_buffer_store_b128(to_frag32(pend), rs, pendo, 0, 16);
      pub_finish(pub_arrive<4>(SY_CNT_C), flags + 8 * FS, tag);
    } else {
      f32x4 rs4[4], mr4[4], cq[2];
      float ck[2], cv[2], rq[4], mq[4];
      unsigned a4, am, ac, ag;
      g1_compute<false>(xbase, wbA, wbB, hw, acc, [&](int i) {
        if (i == 1) { a4 = M_RSTD + (rg * 64 + 4 * g) * 4; asm volatile("" : "+v"(a4)); }
        if (i == 2) { am = M_RSTD + (rg * 64 + m) * 4; asm volatile("" : "+v"(am)); }
        if (i == 3) { ac = M_CS + (cbA * 16 + m) * 4; asm volatile("" : "+v"(ac)); }
        if (i == 4) { ag = M_CS + (cbA * 16 + 4 * g) * 4; asm volatile("" : "+v"(ag)); }
        if (i == 5 || i == 6) { const int hh = 6 - i; const unsigned a = ac ^ (hh * 128); ck[hh] = *(LAS float*)(a + 256); cv[hh] = *(LAS float*)(a + 512); }
        if (i >= 7 && i < 11) { rs4[i - 7] = *(LAS f32x4*)(a4 + (i - 7) * 64); mr4[i - 7] = *(LAS f32x4*)(a4 + (i - 7) * 64 + (M_MR - M_RSTD)); }
        if (i >= 11 && i < 15) { rq[i - 11] = *(LAS float*)(am + (i - 11) * 64); mq[i - 11] = *(LAS float*)(am + (i - 11) * 64 + (M_MR - M_RSTD)); }
        if (i == 15 || i == 16) cq[i - 15] = *(LAS f32x4*)(ag ^ ((i - 15) * 128));
      });
      const u32x4 ones = {0x3F803F80u, 0x3F803F80u, 0x3F803F80u, 0x3F803F80u};
      f32x4 kv, zz;
#pragma unroll
      for (int hh = 1; hh >= 0; --hh) {
        unsigned pk[8], vv[8];
        f32x2 t[8], tv[8];
#pragma unroll
        for (int i = 0; i < 8; ++i) { t[i] = pk_fma(half2(mr4[i >> 1], i & 1), splat(ck[hh]), splat(1e-6f)); tv[i] = half2(mr4[i >> 1], i & 1) * splat(cv[hh]); }
        SB();
        if (hh == 0) {
          *(LAS f32x4*)(M_KV + hw * 2048 + lane * 16) = kv; *(LAS f32x4*)(M_KV + hw * 2048 + 1024 + lane * 16) = zz;
          word_set(SY_P1 + hw, 1u);
        }
#pragma unroll
        for (int i = 0; i < 8; ++i) {
          t[i] = pk_fma(half2(acc[3 * hh + 1][i >> 1], i & 1), half2(rs4[i >> 1], i & 1), t[i]);
          tv[i] = pk_fma(half2(acc[3 * hh + 2][i >> 1], i & 1), half2(rs4[i >> 1], i & 1), tv[i]);
        }
        SB();
#pragma unroll
        for (int i = 0; i < 8; ++i) vv[i] = pack2(tv[i][0], tv[i][1]);
        phi_pack<8>(t, pk);
        kv = f32x4{0, 0, 0, 0}; zz = f32x4{0, 0, 0, 0};
#pragma unroll
        for (int pr = 0; pr < 2; ++pr) {
          const u32x4 pka = {pk[4 * pr], pk[4 * pr + 1], pk[4 * pr + 2], pk[4 * pr + 3]};
          kv = mfma32(pka, u32x4{vv[4 * pr], vv[4 * pr + 1], vv[4 * pr + 2], vv[4 * pr + 3]}, kv);
          zz = mfma32(pka, ones, zz);
        }
        asm volatile("" : "+v"(kv), "+v"(zz));
        SB();
      }
      word_wait(SY_P1 + (hw ^ 2));
      kv += *(LAS f32x4*)(M_KV + (hw ^ 2) * 2048 + lane * 16);
      zz += *(LAS f32x4*)(M_KV + (hw ^ 2) * 2048 + 1024 + lane * 16);
      const int rec = WS_KVP + ((j * 4 + cbA) * 16 + rp) * KVREC;
      __builtin_amdgcn_raw_buffer_store_b128(u32x4{pack2(kv[0], kv[1]), tag32, pack2(kv[2], kv[3]), tag32}, rs, lane * 16 + rec, 0, 16);
      if (m == 0) __builtin_amdgcn_raw_buffer_store_b128(u32x4{pack2(zz[0], zz[1]), tag32, pack2(zz[2], zz[3]), tag32}, rs, 1024 + g * 16 + rec, 0, 16);
      load_res();
      SB();
#pragma unroll
      for (int hh = 0; hh < 2; ++hh) {
        f32x2 t[8];
#pragma unroll
        for (int i = 0; i < 8; ++i) t[i] = pk_fma(splat(mq[i >> 1]), half2(cq[hh], i & 1), splat(1e-6f));
        SB();
#pragma unroll
        for (int i = 0; i < 8; ++i) t[i] = pk_fma(half2(acc[3 * hh][i >> 1], i & 1), splat(rq[i >> 1]), t[i]);
        SB();
        phi_pack<8>(t, pq[hh]);
#pragma unroll
        for (int i = 0; i < 8; ++i) asm volatile("" : "+v"(pq[hh][i]));
      }
      SB();
    }

    f32x16 o2[2];
    unsigned bc[2];
    u32x2 pl[6][4], ph[6][4];
    lvl_wait(SY_LAND, 1u);
    bc[0] = kh * 24576 + ((lane >> 5) * 8 + q4) * 128 + (g & 1) * 32 + 8 * p4 + 2 * STAGE + (((q4 >> 1) ^ kh) << 6);
    bc[1] = bc[0] ^ 64;
#pragma unroll
    for (int i = 0; i < 6; ++i)
#pragma unroll
      for (int q = 0; q < 4; ++q) { pl[i][q] = trread(bc[q & 1] + i * 4096 + (q >> 1) * 2048); ph[i][q] = trread(bc[q & 1] + i * 4096 + (q >> 1) * 2048 + 512); }
    word_set(SY_CONS + hw, 1u);
    word_set(SY_PRE + hw, ~tag32);
    SB();
    if (!ht) { kreq(0x1ffffu); asm volatile("s_waitcnt vmcnt(17)" ::: "memory"); }
    asm volatile("" : "+v"(xres[0]), "+v"(xres[1]), "+v"(b2v[0]), "+v"(b2v[1]) : "v"(wrm));
#pragma unroll
    for (int i = 0; i < 2; ++i)
#pragma unroll
      for (int d = 0; d < 2; ++d) {
        const auto p = __builtin_amdgcn_permlane32_swap(xres[i][d], xres[i][2 + d], false, false);
        const auto q = __builtin_amdgcn_permlane32_swap(b2v[i][d], b2v[i][2 + d], false, false);
#pragma unroll
        for (int e = 0; e < 2; ++e) {
          o2[0][8 * i + 4 * e + 2 * d] = bflo(p[e]) + bflo(q[e]); o2[0][8 * i + 4 * e + 2 * d + 1] = bfhi(p[e]) + bfhi(q[e]);
          o2[1][8 * i + 4 * e + 2 * d] = o2[1][8 * i + 4 * e + 2 * d + 1] = 0.f;
        }
      }
    asm volatile("" : "+v"(o2[0]));
    int ah = WS_A + ((rt * 2 + rh) * 144 + 72 * kh) * 1024;
    asm volatile("" : "+s"(ah));
    if (!ht) {
      f32x4 sm = {0.f, 0.f, 0.f, 0.f};
      unsigned np = 0;
      auto stale = [&](int i) {
        u64 c; unsigned bit;
        asm volatile("v_cmp_ne_u32 vcc, %2, %3\n\tv_cmp_ne_u32 %1, %2, %4\n\ts_or_b64 %1, %1, vcc\n\ts_cselect_b32 %0, %5, 0"
                     : "=s"(bit), "=&s"(c) : "s"(tag32), "v"(kt[i].y), "v"(kt[i].w), "i"(1 << i) : "vcc", "scc");
        return bit;
      };
      auto add = [&](int i) { sm += f32x4{bflo(kt[i].x), bfhi(kt[i].x), bflo(kt[i].z), bfhi(kt[i].z)}; };
#pragma unroll
      for (int i = 0; i < 17; ++i) {
        asm volatile("s_waitcnt vmcnt(%1)" : "+v"(kt[i]) : "i"(16 - i) : "memory");
        np |= stale(i);
        if (i < 16) add(i);
        asm volatile("" : "+v"(sm));
      }
      if (__builtin_expect(np != 0u, 0)) {
#pragma unroll 1
        for (unsigned spins = 0; np && spins < SPIN_CAP; ++spins) {
          kreq(np);
          asm volatile("s_waitcnt vmcnt(0)" ::: "memory");
          np = 0;
#pragma unroll
          for (int i = 0; i < 17; ++i) { asm volatile("" : "+v"(kt[i])); np |= stale(i); }
        }
        sm = f32x4{0.f, 0.f, 0.f, 0.f};
#pragma unroll
        for (int i = 0; i < 16; ++i) add(i);
      }
      f32x4 z = {bflo(kt[16].x), bfhi(kt[16].x), bflo(kt[16].z), bfhi(kt[16].z)};
#define ROR4(N) ROR1("%0", N) ROR1("%1", N) ROR1("%2", N) ROR1("%3", N)
      asm volatile("s_nop 1\n\t" ROR4(8) ROR4(4) ROR4(2) ROR4(1) : "+v"(z[0]), "+v"(z[1]), "+v"(z[2]), "+v"(z[3]));
      u32x4 own = {pack2(sm[0], sm[1]), pack2(sm[2], sm[3]), pack2(z[0], z[1]), pack2(z[2], z[3])};
      *(LAS u32x4*)(M_KV + (hw ^ 2) * 2048 + lane * 16) = own;
      word_set(SY_P2 + hw, 1u);
      unsigned pfl;
      u32x4 oth;
#pragma unroll
      for (int hh = 0; hh < 2; ++hh) {
        if (hh == 1) {
          if (!__builtin_amdgcn_readfirstlane(pfl)) { word_wait(SY_P2 + (hw ^ 2)); oth = *(LAS u32x4*)(M_KV + hw * 2048 + lane * 16); }
          own = oth;
        }
        f32x4 num[4], den[4];
#pragma unroll
        for (int rb = 0; rb < 4; ++rb) {
          const u32x2 pf = {pq[hh][2 * rb], pq[hh][2 * rb + 1]};
          den[rb] = mfma16(u32x2{own.z, own.w}, pf, f32x4{0, 0, 0, 0});
          num[rb] = mfma16(u32x2{own.x, own.y}, pf, f32x4{0, 0, 0, 0});
        }
        SB();
#pragma unroll
        for (int pr = 0; pr < 2; ++pr) {
          u32x4 o;
          if (hh == 0 && pr == 1) {
            pfl = *(volatile LAS unsigned*)(M_SY + (SY_P2 + (hw ^ 2)) * 4);
            oth = *(volatile LAS u32x4*)(M_KV + hw * 2048 + lane * 16);
          }
#pragma unroll
          for (int h2 = 0; h2 < 2; ++h2) {
            const int rb = pr * 2 + h2;
            const f32x4 v = num[rb] * __builtin_amdgcn_rcpf(den[rb][0] + 1e-6f);
            o[h2 * 2] = pack2(v[0], v[1]); o[h2 * 2 + 1] = pack2(v[2], v[3]);
          }
          __builtin_amdgcn_raw_buffer_store_b128(to_frag32(o), rs, lane * 16 + WS_A + ((P0 + pr) * 144 + (j >= 4 ? 112 : 56) + j * 4 + (hh ? cbB : cbA)) * 1024, 0, 16);
          SB();
        }
      }
    }
    char* const fo = flags + (ht ? 16 : 24) * FS;
    const bool late = min4(*(volatile LAS u32x4*)(M_SY + SY_LVL * 4)) >= 1u;
    CFENCE();
    if (!late) { pub_finish(pub_arrive<0>(SY_CNT_B), fo, tag); lvl_wait<1>(SY_LVL, 1u); }
    u32x4 aw[6][2];
#pragma unroll
    for (int n = 0; n < 12; ++n) aw[n >> 1][n & 1] = __builtin_amdgcn_raw_buffer_load_b128(rs, lane * 16, ah + n * 1024, 16);
    SB();
    unsigned oldl = 0;
    if (late) oldl = pub_arrive<12>(SY_CNT_B);
    u32x2 wl[2][4], wh[2][4];
    unsigned gate = 2, lm;
    u32x4 lv;
    const unsigned bn = kh * 24576 + (p4 >> 1) * 256 + (p4 & 1) * 1024 + (((((lane >> 5) ^ p4) & 1) << 4) | (q4 << 2) | (((kh ^ (p4 >> 1)) & 1) << 1) | (g & 1)) * 8;
    auto six = [&](auto modec, int delta, bool sw) {
      constexpr bool FIRST = decltype(modec)::value == 0, LAST = decltype(modec)::value == 2;
#pragma unroll
      for (int i = 0; i < 6; ++i) {
        if (i == 2) {
          if (__builtin_expect(lm < gate, 0)) lvl_wait(SY_LVL, gate);
          CFENCE();
          ++gate;
        }
        if (FIRST && i == 1) pub_finish(oldl, fo, tag);
        if (i == 5) { if (sw) { bc[0] = bn; bc[1] = bn ^ 16; } else { bc[0] += delta; bc[1] += delta; } }
        SB();
#pragma unroll
        for (int q = 0; q < 4; ++q) {
          const int cur = i & 1;
          const u32x4 wf = FIRST ? u32x4{pl[i][q].x, pl[i][q].y, ph[i][q].x, ph[i][q].y} : u32x4{wl[cur][q].x, wl[cur][q].y, wh[cur][q].x, wh[cur][q].y};
          o2[q & 1] = mfma32x32(wf, aw[i][q >> 1], o2[q & 1]);
          SB();
          if (FIRST ? i == 5 : (i < 5 || !LAST)) {
#pragma unroll
            for (int e = 0; e < 8; ++e) if (e / 3 == q) {
              const unsigned a = bc[(e >> 1) & 1] + ((i + 1) % 6) * 4096 + (e >> 2) * 2048;
              if (e & 1) wh[cur ^ 1][e >> 1] = trread(a + 512); else wl[cur ^ 1][e >> 1] = trread(a);
            }
          }
          if (i == 0 && q == 3) lv = *(volatile LAS u32x4*)(M_SY + SY_LVL * 4);
          if (i == 1 && q == 3) { word_set(SY_CONS + hw, gate); lm = min4(lv); }
          if (!LAST && (q & 1)) aw[i][q >> 1] = __builtin_amdgcn_raw_buffer_load_b128(rs, lane * 16, ah + (2 * (i + 6) + (q >> 1)) * 1024, 16);
          SB();
        }
      }
      ah += 12 * 1024;
    };
    six(std::integral_constant<int, 0>{}, -2 * STAGE, false);
#pragma unroll 1
    for (int it = 1; it < 5; ++it) six(std::integral_constant<int, 1>{}, STAGE, it == 3);
    six(std::integral_constant<int, 2>{}, 0, false);
    asm volatile("" : "+v"(o2[0]), "+v"(o2[1]));
    const unsigned xo = 2 * STAGE + rh * 4096 + lane * 16;
#pragma unroll
    for (int i = 0; i < 4; ++i) *(LAS f32x4*)(xo + kh * 24576 + i * 1024) = f32x4{o2[1][4 * i], o2[1][4 * i + 1], o2[1][4 * i + 2], o2[1][4 * i + 3]};
    word_set(SY_P3 + hw, 1u);
    word_wait(SY_P3 + (hw ^ 2));
    f32x4 t[4];
#pragma unroll
    for (int i = 0; i < 4; ++i) t[i] = *(LAS f32x4*)(xo + (1 - kh) * 24576 + i * 1024);
    SB();
#pragma unroll
    for (int i = 0; i < 2; ++i) {
      unsigned o[4];
#pragma unroll
      for (int e = 0; e < 4; ++e) o[e] = pack2(o2[0][8 * i + 2 * e] + t[2 * i + (e >> 1)][2 * (e & 1)], o2[0][8 * i + 2 * e + 1] + t[2 * i + (e >> 1)][2 * (e & 1) + 1]);
      const auto p = __builtin_amdgcn_permlane32_swap(o[0], o[2], false, false);
      const auto q = __builtin_amdgcn_permlane32_swap(o[1], o[3], false, false);
      *(GAS u32x4*)(out + ooff + i * 32) = u32x4{p[0], q[0], p[1], q[1]};
    }
  }
}

extern "C" void kernel_launch(void* const* d_in, const int* in_sizes, int n_in, void* d_out, int out_size, void* d_ws, size_t ws_size,
                              hipStream_t stream) {
  (void)in_sizes; (void)n_in; (void)out_size; (void)ws_size;
  (void)hipFuncSetAttribute((const void*)fused_block_kernel, hipFuncAttributeMaxDynamicSharedMemorySize, LDS_BYTES);
  fused_block_kernel<<<256, 512, LDS_BYTES, stream>>>((const char*)d_in[0], (const char*)d_in[1], (const char*)d_in[2], (const char*)d_in[4],
                                                     (const char*)d_in[5], (const char*)d_in[6], (const char*)d_in[7], (char*)d_out, (char*)d_ws);
}
```
